```python
import torch

def _softmax(x, axis=-1):
    return torch.softmax(x, dim=axis)
def _gelu(x):
    return torch.nn.functional.gelu(x, approximate="tanh")
def _silu(x):
    return torch.nn.functional.silu(x)
def _sigmoid(x):
    return torch.sigmoid(x)
def _layernorm(x, axis=-1, eps=1e-5):
    m = x.mean(dim=axis, keepdim=True)
    v = x.var(dim=axis, keepdim=True, unbiased=False)
    return (x - m) / torch.sqrt(v + eps)
def _scatter_add(tgt, idx, src, axis=0):
    return tgt.clone().index_add_(axis, idx, src)
def _segment_sum(src, seg, n):
    out = torch.zeros((n,) + src.shape[1:], dtype=src.dtype)
    return out.index_add_(0, seg, src)
def _pad(x, pw):
    flat = tuple(v for pair in reversed(pw) for v in pair)
    return torch.nn.functional.pad(x, flat)

kernel_name = 'standalone__mha_block_cached__near_ridge'

def setup_inputs():

    _w = (lambda a: (lambda t: t.to(torch.bfloat16) if t.is_floating_point() else t)(torch.as_tensor(a)))
    B, Sq, D = 8, 128, 576
    H, Sk, Dh = 32, 1024, 128
    return {
        "x": _w((lambda *s: torch.randn(*s, dtype=torch.bfloat16) * 0.8)(B, Sq, D)),
        "k_cache": _w((lambda *s: torch.randn(*s, dtype=torch.bfloat16) * 0.8)(B, H, Sk, Dh)),
        "v_cache": _w((lambda *s: torch.randn(*s, dtype=torch.bfloat16) * 0.05)(B, H, Sk, Dh)),
        "wq": _w((lambda *s: torch.randn(*s, dtype=torch.bfloat16) * 0.8)(D, H * Dh) * (D ** -0.5)),
        "wo": _w((lambda *s: torch.randn(*s, dtype=torch.bfloat16) * 0.05)(H * Dh, D) * ((H * Dh) ** -0.5)),
    }

def reference(x, k_cache, v_cache, wq, wo):
    B, Sq, D = x.shape
    H, Dh = 32, 128
    q = (lambda x, *d: x.permute(*d))((x @ wq).reshape(B, Sq, H, Dh), 0, 2, 1, 3)
    scores = torch.einsum("bhqd,bhkd->bhqk", q, k_cache) * (Dh ** -0.5)
    p = _softmax(scores, axis=-1)
    o = torch.einsum("bhqk,bhkd->bhqd", p, v_cache)
    return (lambda x, *d: x.permute(*d))(o, 0, 2, 1, 3).reshape(B, Sq, H * Dh) @ wo
```

```cpp
#include <hip/hip_runtime.h>
#include <hip/hip_bf16.h>

constexpr int NB = 8, SQ = 128, DM = 576, NH = 32, SK = 1024, DH = 128;
constexpr int HD = NH * DH;

using bf16x8 = __attribute__((ext_vector_type(8))) short;
using s16x4 = __attribute__((ext_vector_type(4))) short;
using f32x16 = __attribute__((ext_vector_type(16))) float;
using f32x4 = __attribute__((ext_vector_type(4))) float;
using u32x4 = __attribute__((ext_vector_type(4))) unsigned;
using u32x2 = __attribute__((ext_vector_type(2))) unsigned;
typedef __attribute__((address_space(3))) s16x4* lds_s16x4_ptr;
typedef __attribute__((address_space(3))) void* lds_void_ptr;

#define MFMA32(a, b, c) __builtin_amdgcn_mfma_f32_32x32x16_bf16((a), (b), (c), 0, 0, 0)
#define FENCE() asm volatile("" ::: "memory")
#define BARRIER() do { asm volatile("s_waitcnt lgkmcnt(0)" ::: "memory"); __builtin_amdgcn_s_barrier(); FENCE(); } while (0)

__device__ __forceinline__ unsigned cvtpk(float lo, float hi) {
  unsigned r; asm volatile("v_cvt_pk_bf16_f32 %0, %1, %2" : "=v"(r) : "v"(lo), "v"(hi)); return r;
}
__device__ __forceinline__ bf16x8 pack8(const f32x16& x, int s) {
  u32x4 p;
  p[0] = cvtpk(x[8 * s + 0], x[8 * s + 1]); p[1] = cvtpk(x[8 * s + 2], x[8 * s + 3]);
  p[2] = cvtpk(x[8 * s + 4], x[8 * s + 5]); p[3] = cvtpk(x[8 * s + 6], x[8 * s + 7]);
  return __builtin_bit_cast(bf16x8, p);
}
__device__ __forceinline__ s16x4 trread(unsigned addr) {
  return __builtin_amdgcn_ds_read_tr16_b64_v4i16((lds_s16x4_ptr)(uintptr_t)addr);
}
__device__ __forceinline__ void glds16(const void* g, unsigned lds_addr) {
  __builtin_amdgcn_global_load_lds((const unsigned*)g, (lds_void_ptr)(uintptr_t)lds_addr, 16, 0, 0);
}

constexpr int KV_SLOT = 32768, RING = 4;
constexpr int WQ_BASE = KV_SLOT * RING;
constexpr int LDS_BYTES = WQ_BASE + 2 * 16384;
constexpr int NT = SK / 64;
constexpr int NCH = DM / 64;

__global__ __launch_bounds__(512, 1) void attn_kernel(const short* __restrict__ x, const short* __restrict__ kc,
                                                      const short* __restrict__ vc, const short* __restrict__ wq,
                                                      short* __restrict__ ows) {
  extern __shared__ __attribute__((aligned(16))) char smem[];
  const unsigned lds0 = (unsigned)(uintptr_t)smem;
  const int tid = threadIdx.x, lane = tid & 63;
  const int wid = __builtin_amdgcn_readfirstlane(tid >> 6);
  const int bid = blockIdx.x;
  const int h = (bid & 7) * 4 + ((bid >> 3) & 3), b = bid >> 5;

  if (wid >= 4) {
    const int lw = wid - 4;
    const long kvbase = ((long)(b * NH + h)) * SK * DH;
    const int rloc = lane >> 4, chp = lane & 15;
    const short* ksrc[4]; const short* vsrc[4];
#pragma unroll
    for (int i = 0; i < 4; ++i) {
      const int row = (lw * 4 + i) * 4 + rloc;
      ksrc[i] = kc + kvbase + row * DH + ((chp ^ (row & 15)) * 8);
      vsrc[i] = vc + kvbase + row * DH + ((chp ^ ((row & 3) << 2)) * 8);
    }
    auto issue = [&](int t) {
      const unsigned dst = lds0 + (t & 3) * KV_SLOT + lw * 4096;
#pragma unroll
      for (int i = 0; i < 4; ++i) glds16(ksrc[i] + t * 64 * DH, dst + i * 1024);
#pragma unroll
      for (int i = 0; i < 4; ++i) glds16(vsrc[i] + t * 64 * DH, dst + 16384 + i * 1024);
    };
    issue(0); issue(1); issue(2); issue(3);
#pragma unroll 1
    for (int i = 0; i < 2 * NCH; ++i) { __builtin_amdgcn_s_barrier(); }
    FENCE();
    asm volatile("s_waitcnt vmcnt(24)" ::: "memory"); __builtin_amdgcn_s_barrier(); FENCE();
#pragma unroll 1
    for (int t = 1; t <= 12; ++t) {
      asm volatile("s_waitcnt vmcnt(16)" ::: "memory"); __builtin_amdgcn_s_barrier(); FENCE();
      issue(t + 3);
    }
    asm volatile("s_waitcnt vmcnt(16)" ::: "memory"); __builtin_amdgcn_s_barrier(); FENCE();
    asm volatile("s_waitcnt vmcnt(8)" ::: "memory"); __builtin_amdgcn_s_barrier(); FENCE();
    asm volatile("s_waitcnt vmcnt(0)" ::: "memory"); __builtin_amdgcn_s_barrier(); FENCE();
    return;
  }

  const int r32 = lane & 31, hh = lane >> 5;
  const int g1 = (lane >> 4) & 1, q4 = (lane >> 2) & 3, p4 = lane & 3;
  bf16x8 qf[8];
  {
    const int rloc = lane >> 4, chp = lane & 15;
    const short* wsrc[4];
#pragma unroll
    for (int i = 0; i < 4; ++i) {
      const int row = (wid * 4 + i) * 4 + rloc;
      wsrc[i] = wq + (long)row * HD + h * DH + ((chp ^ ((row & 3) << 2)) * 8);
    }
    auto issuew = [&](int c) {
      const unsigned dst = lds0 + WQ_BASE + (c & 1) * 16384 + wid * 4096;
#pragma unroll
      for (int i = 0; i < 4; ++i) glds16(wsrc[i] + (long)c * 64 * HD, dst + i * 1024);
    };
    const short* xsrc = x + ((long)(b * SQ + wid * 32 + r32)) * DM + hh * 8;
    bf16x8 xf[4], xn[4];
#pragma unroll
    for (int s = 0; s < 4; ++s) xf[s] = *(const bf16x8*)(xsrc + s * 16);
    issuew(0); issuew(1);
    f32x16 acc[4] = {};
    const int psw = ((p4 & 1) << 1) | (p4 >> 1);
    const unsigned wrd = (unsigned)((8 * hh + q4) * 256 + (((2 * g1 + (psw >> 1)) ^ (q4 << 2)) * 16) + (psw & 1) * 8);
#pragma unroll 1
    for (int c = 0; c < NCH; ++c) {
      if (c < NCH - 1) asm volatile("s_waitcnt vmcnt(4)" ::: "memory");
      else asm volatile("s_waitcnt vmcnt(0)" ::: "memory");
      __builtin_amdgcn_s_barrier(); FENCE();
      if (c + 1 < NCH) {
#pragma unroll
        for (int s = 0; s < 4; ++s) xn[s] = *(const bf16x8*)(xsrc + (c + 1) * 64 + s * 16);
      }
      const unsigned wb = lds0 + WQ_BASE + (c & 1) * 16384 + wrd;
#pragma unroll
      for (int s = 0; s < 4; ++s) {
#pragma unroll
        for (int db = 0; db < 4; ++db) {
          const unsigned a0 = wb + s * 16 * 256;
          s16x4 lo = trread((a0 ^ (db * 64)) );
          s16x4 hi = trread((a0 ^ (db * 64)) + 4 * 256);
          bf16x8 a = __builtin_shufflevector(lo, hi, 0, 1, 2, 3, 4, 5, 6, 7);
          acc[db] = MFMA32(a, xf[s], acc[db]);
        }
      }
      BARRIER();
      if (c + 2 < NCH) issuew(c + 2);
#pragma unroll
      for (int s = 0; s < 4; ++s) xf[s] = xn[s];
    }
#pragma unroll
    for (int db = 0; db < 4; ++db) { qf[2 * db] = pack8(acc[db], 0); qf[2 * db + 1] = pack8(acc[db], 1); }
  }

  constexpr float C = 0.088388347648318440f * 1.4426950408889634f;
  float m = -1e30f, l = 0.f;
  f32x16 o[4] = {};
  const unsigned krd = (unsigned)(r32 * 256);
  const int kx = r32 & 15;
  const unsigned vrd = (unsigned)((4 * hh + q4) * 256 + (((2 * g1 + (p4 >> 1)) ^ (q4 << 2)) * 16) + (p4 & 1) * 8);
#pragma unroll 1
  for (int t = 0; t < NT; ++t) {
    BARRIER();
    const unsigned kb0 = lds0 + (t & 3) * KV_SLOT;
    f32x16 s0 = {}, s1 = {};
#pragma unroll
    for (int s = 0; s < 8; ++s) {
      const unsigned ca = (unsigned)(((2 * s + hh) ^ kx) * 16);
      bf16x8 a0 = *(const __attribute__((address_space(3))) bf16x8*)(uintptr_t)(kb0 + krd + ca);
      bf16x8 a1 = *(const __attribute__((address_space(3))) bf16x8*)(uintptr_t)(kb0 + krd + 32 * 256 + ca);
      s0 = MFMA32(a0, qf[s], s0);
      s1 = MFMA32(a1, qf[s], s1);
    }
    float pm = s0[0];
#pragma unroll
    for (int r = 1; r < 16; ++r) pm = fmaxf(pm, s0[r]);
#pragma unroll
    for (int r = 0; r < 16; ++r) pm = fmaxf(pm, s1[r]);
    pm = fmaxf(pm, __shfl_xor(pm, 32));
    const float mn = fmaxf(m, pm);
    const float alpha = __builtin_amdgcn_exp2f((m - mn) * C);
    m = mn;
    const float mc = -mn * C;
    float ps = 0.f;
#pragma unroll
    for (int r = 0; r < 16; ++r) { s0[r] = __builtin_amdgcn_exp2f(fmaf(s0[r], C, mc)); ps += s0[r]; }
#pragma unroll
    for (int r = 0; r < 16; ++r) { s1[r] = __builtin_amdgcn_exp2f(fmaf(s1[r], C, mc)); ps += s1[r]; }
    l = l * alpha + ps;
#pragma unroll
    for (int db = 0; db < 4; ++db)
#pragma unroll
      for (int r = 0; r < 16; ++r) o[db][r] *= alpha;
    bf16x8 pf[4];
    pf[0] = pack8(s0, 0); pf[1] = pack8(s0, 1); pf[2] = pack8(s1, 0); pf[3] = pack8(s1, 1);
    const unsigned vb0 = kb0 + 16384 + vrd;
#pragma unroll
    for (int db = 0; db < 4; ++db) {
#pragma unroll
      for (int ks = 0; ks < 4; ++ks) {
        const unsigned a0 = (vb0 + ks * 16 * 256) ^ (db * 64);
        s16x4 lo = trread(a0);
        s16x4 hi = trread(a0 + 8 * 256);
        bf16x8 a = __builtin_shufflevector(lo, hi, 0, 1, 2, 3, 4, 5, 6, 7);
        o[db] = MFMA32(a, pf[ks], o[db]);
      }
    }
  }
  l += __shfl_xor(l, 32);
  const float rl = 1.0f / l;
  short* orow = ows + ((long)(b * SQ + wid * 32 + r32)) * HD + h * DH + 4 * hh;
#pragma unroll
  for (int db = 0; db < 4; ++db) {
#pragma unroll
    for (int g = 0; g < 4; ++g) {
      u32x2 w;
      w[0] = cvtpk(o[db][4 * g + 0] * rl, o[db][4 * g + 1] * rl);
      w[1] = cvtpk(o[db][4 * g + 2] * rl, o[db][4 * g + 3] * rl);
      *(u32x2*)(orow + db * 32 + g * 8) = w;
    }
  }
}

__global__ __launch_bounds__(256) void oproj_kernel(const short* __restrict__ ows, const short* __restrict__ wo,
                                                    short* __restrict__ out) {
  __shared__ __attribute__((aligned(16))) char smem[64 * 128];
  const unsigned lds0 = (unsigned)(uintptr_t)smem;
  const int tid = threadIdx.x, lane = tid & 63, wid = tid >> 6;
  const int m0 = blockIdx.x * 64 + (wid >> 1) * 32, n0 = blockIdx.y * 64, nw = (wid & 1) * 32;
  const int r32 = lane & 31, hh = lane >> 5, g1 = (lane >> 4) & 1, q4 = (lane >> 2) & 3, p4 = lane & 3;
  f32x16 acc = {};
  const short* asrc = ows + (long)(m0 + r32) * HD + hh * 8;
  for (int k0 = 0; k0 < HD; k0 += 64) {
    __syncthreads();
    {
#pragma unroll
      for (int i = 0; i < 2; ++i) {
        const int idx = tid + i * 256, row = idx >> 3, ch = idx & 7;
        *(u32x4*)(smem + row * 128 + ch * 16) = *(const u32x4*)(wo + (long)(k0 + row) * DM + n0 + ch * 8);
      }
    }
    __syncthreads();
#pragma unroll
    for (int s = 0; s < 4; ++s) {
      bf16x8 a = *(const bf16x8*)(asrc + k0 + s * 16);
      const unsigned ad = lds0 + (s * 16 + 8 * hh + q4) * 128 + (nw + 16 * g1 + 4 * p4) * 2;
      s16x4 lo = trread(ad), hi = trread(ad + 4 * 128);
      bf16x8 bb = __builtin_shufflevector(lo, hi, 0, 1, 2, 3, 4, 5, 6, 7);
      acc = MFMA32(a, bb, acc);
    }
  }
#pragma unroll
  for (int r = 0; r < 16; ++r) {
    const int row = (r & 3) + 8 * (r >> 2) + 4 * hh;
    __hip_bfloat16 v = __float2bfloat16(acc[r]);
    out[(long)(m0 + row) * DM + n0 + nw + r32] = *(short*)&v;
  }
}

extern "C" void kernel_launch(void* const* d_in, const int* in_sizes, int n_in, void* d_out, int out_size, void* d_ws,
                              size_t ws_size, hipStream_t stream) {
  const short* x = (const short*)d_in[0];
  const short* kc = (const short*)d_in[1];
  const short* vc = (const short*)d_in[2];
  const short* wq = (const short*)d_in[3];
  const short* wo = (const short*)d_in[4];
  short* ows = (short*)d_ws;
  hipFuncSetAttribute((const void*)attn_kernel, hipFuncAttributeMaxDynamicSharedMemorySize, LDS_BYTES);
  attn_kernel<<<NB * NH, 512, LDS_BYTES, stream>>>(x, kc, vc, wq, ows);
  oproj_kernel<<<dim3(16, 9), 256, 0, stream>>>(ows, wo, (short*)d_out);
}
```

```cpp
#include <hip/hip_runtime.h>
#include <hip/hip_bf16.h>

constexpr int NB = 8, SQ = 128, DM = 576, NH = 32, SK = 1024, DH = 128;
constexpr int HD = NH * DH;

using bf16x8 = __attribute__((ext_vector_type(8))) short;
using s16x4 = __attribute__((ext_vector_type(4))) short;
using f32x16 = __attribute__((ext_vector_type(16))) float;
using f32x4 = __attribute__((ext_vector_type(4))) float;
using u32x4 = __attribute__((ext_vector_type(4))) unsigned;
using u32x2 = __attribute__((ext_vector_type(2))) unsigned;
typedef __attribute__((address_space(3))) s16x4* lds_s16x4_ptr;
typedef __attribute__((address_space(3))) void* lds_void_ptr;

#define MFMA32(a, b, c) __builtin_amdgcn_mfma_f32_32x32x16_bf16((a), (b), (c), 0, 0, 0)
#define FENCE() asm volatile("" ::: "memory")
#define BARRIER() do { asm volatile("s_waitcnt lgkmcnt(0)" ::: "memory"); __builtin_amdgcn_s_barrier(); FENCE(); } while (0)

__device__ __forceinline__ unsigned cvtpk(float lo, float hi) {
  typedef __attribute__((ext_vector_type(2))) __bf16 bf2;
  bf2 v = {(__bf16)lo, (__bf16)hi}; return __builtin_bit_cast(unsigned, v);
}
__device__ __forceinline__ bf16x8 pack8(const f32x16& x, int s) {
  u32x4 p;
  p[0] = cvtpk(x[8 * s + 0], x[8 * s + 1]); p[1] = cvtpk(x[8 * s + 2], x[8 * s + 3]);
  p[2] = cvtpk(x[8 * s + 4], x[8 * s + 5]); p[3] = cvtpk(x[8 * s + 6], x[8 * s + 7]);
  return __builtin_bit_cast(bf16x8, p);
}
__device__ __forceinline__ s16x4 trread(unsigned addr) {
  return __builtin_amdgcn_ds_read_tr16_b64_v4i16((lds_s16x4_ptr)(uintptr_t)addr);
}
__device__ __forceinline__ void glds16(const void* g, unsigned lds_addr) {
  __builtin_amdgcn_global_load_lds((const unsigned*)g, (lds_void_ptr)(uintptr_t)lds_addr, 16, 0, 0);
}

constexpr int KV_SLOT = 32768, RING = 4;
constexpr int WQ_BASE = KV_SLOT * RING;
constexpr int LDS_BYTES = WQ_BASE + 2 * 16384;
constexpr int NT = SK / 64;
constexpr int NCH = DM / 64;

__global__ __launch_bounds__(512, 1) void attn_kernel(const short* __restrict__ x, const short* __restrict__ kc,
                                                      const short* __restrict__ vc, const short* __restrict__ wq,
                                                      short* __restrict__ ows) {
  extern __shared__ __attribute__((aligned(16))) char smem[];
  const unsigned lds0 = (unsigned)(uintptr_t)smem;
  const int tid = threadIdx.x, lane = tid & 63;
  const int wid = __builtin_amdgcn_readfirstlane(tid >> 6);
  const int bid = blockIdx.x;
  const int h = (bid & 7) * 4 + ((bid >> 3) & 3), b = bid >> 5;

  if (wid >= 4) {
    const int lw = wid - 4;
    const long kvbase = ((long)(b * NH + h)) * SK * DH;
    const int rloc = lane >> 4, chp = lane & 15;
    const short* ksrc[4]; const short* vsrc[4];
#pragma unroll
    for (int i = 0; i < 4; ++i) {
      const int row = (lw * 4 + i) * 4 + rloc;
      ksrc[i] = kc + kvbase + row * DH + ((chp ^ (row & 15)) * 8);
      vsrc[i] = vc + kvbase + row * DH + ((chp ^ ((row & 3) << 2)) * 8);
    }
    const int rot = (bid * 5) & 15;
    auto issue = [&](int t) {
      const unsigned dst = lds0 + (t & 3) * KV_SLOT + lw * 4096;
#pragma unroll
      for (int i = 0; i < 4; ++i) glds16(ksrc[i] + ((t + rot) & 15) * 64 * DH, dst + i * 1024);
#pragma unroll
      for (int i = 0; i < 4; ++i) glds16(vsrc[i] + ((t + rot) & 15) * 64 * DH, dst + 16384 + i * 1024);
    };
    {
      const short* wsrc[4];
#pragma unroll
      for (int i = 0; i < 4; ++i) {
        const int row = (lw * 4 + i) * 4 + rloc;
        wsrc[i] = wq + (long)row * HD + h * DH + ((chp ^ ((row & 3) << 2)) * 8);
      }
#pragma unroll
      for (int c = 0; c < NCH; ++c) {
        const unsigned dst = lds0 + c * 16384 + lw * 4096;
#pragma unroll
        for (int i = 0; i < 4; ++i) glds16(wsrc[i] + (long)c * 64 * HD, dst + i * 1024);
      }
    }
#define LB(n) asm volatile("s_waitcnt vmcnt(" #n ")" ::: "memory"); __builtin_amdgcn_s_barrier(); FENCE();
    LB(32) LB(28) LB(24) issue(0);
    LB(28) LB(24) issue(1);
    LB(28) LB(24) issue(2);
    LB(28) LB(24) issue(3);
    asm volatile("s_waitcnt vmcnt(24)" ::: "memory"); __builtin_amdgcn_s_barrier(); FENCE();
#pragma unroll 1
    for (int t = 1; t <= 12; ++t) {
      asm volatile("s_waitcnt vmcnt(16)" ::: "memory"); __builtin_amdgcn_s_barrier(); FENCE();
      issue(t + 3);
    }
    asm volatile("s_waitcnt vmcnt(16)" ::: "memory"); __builtin_amdgcn_s_barrier(); FENCE();
    asm volatile("s_waitcnt vmcnt(8)" ::: "memory"); __builtin_amdgcn_s_barrier(); FENCE();
    asm volatile("s_waitcnt vmcnt(0)" ::: "memory"); __builtin_amdgcn_s_barrier(); FENCE();
    return;
  }

  const int r32 = lane & 31, hh = lane >> 5;
  const int g1 = (lane >> 4) & 1, q4 = (lane >> 2) & 3, p4 = lane & 3;
  bf16x8 qf[8];
  {
    const short* xsrc = x + ((long)(b * SQ + wid * 32 + r32)) * DM + hh * 8;
    bf16x8 xq[3][4];
#pragma unroll
    for (int c = 0; c < 3; ++c)
#pragma unroll
      for (int s = 0; s < 4; ++s) xq[c][s] = *(const bf16x8*)(xsrc + c * 64 + s * 16);
    f32x16 acc[4] = {};
    const int psw = ((p4 & 1) << 1) | (p4 >> 1);
    const unsigned wrd = (unsigned)((8 * hh + q4) * 256 + (((2 * g1 + (psw >> 1)) ^ (q4 << 2)) * 16) + (psw & 1) * 8);
#pragma unroll
    for (int c = 0; c < NCH; ++c) {
      BARRIER();
      bf16x8 xf[4];
#pragma unroll
      for (int s = 0; s < 4; ++s) xf[s] = xq[c % 3][s];
      if (c + 3 < NCH) {
#pragma unroll
        for (int s = 0; s < 4; ++s) xq[c % 3][s] = *(const bf16x8*)(xsrc + (c + 3) * 64 + s * 16);
      }
      const unsigned wb = lds0 + c * 16384 + wrd;
      s16x4 wl[16], wh[16];
#pragma unroll
      for (int s = 0; s < 4; ++s) {
#pragma unroll
        for (int db = 0; db < 4; ++db) {
          const unsigned a0 = wb + s * 16 * 256;
          wl[s * 4 + db] = trread((a0 ^ (db * 64)));
          wh[s * 4 + db] = trread((a0 ^ (db * 64)) + 4 * 256);
        }
      }
      __builtin_amdgcn_sched_barrier(0);
#pragma unroll
      for (int s = 0; s < 4; ++s) {
#pragma unroll
        for (int db = 0; db < 4; ++db) {
          bf16x8 a = __builtin_shufflevector(wl[s * 4 + db], wh[s * 4 + db], 0, 1, 2, 3, 4, 5, 6, 7);
          acc[db] = MFMA32(a, xf[s], acc[db]);
        }
      }
    }
#pragma unroll
    for (int db = 0; db < 4; ++db) { qf[2 * db] = pack8(acc[db], 0); qf[2 * db + 1] = pack8(acc[db], 1); }
  }

  constexpr float C = 0.088388347648318440f * 1.4426950408889634f;
  float m = -1e30f, l = 0.f;
  f32x16 o[4] = {};
  const unsigned krd = (unsigned)(r32 * 256);
  const int kx = r32 & 15;
  const unsigned vrd = (unsigned)((4 * hh + q4) * 256 + (((2 * g1 + (p4 >> 1)) ^ (q4 << 2)) * 16) + (p4 & 1) * 8);
#pragma unroll 1
  for (int t = 0; t < NT; ++t) {
    BARRIER();
    const unsigned kb0 = lds0 + (t & 3) * KV_SLOT;
    f32x16 s0 = {}, s1 = {};
    bf16x8 kf[16];
#pragma unroll
    for (int s = 0; s < 8; ++s) {
      const unsigned ca = (unsigned)(((2 * s + hh) ^ kx) * 16);
      kf[2 * s] = *(const __attribute__((address_space(3))) bf16x8*)(uintptr_t)(kb0 + krd + ca);
      kf[2 * s + 1] = *(const __attribute__((address_space(3))) bf16x8*)(uintptr_t)(kb0 + krd + 32 * 256 + ca);
    }
    __builtin_amdgcn_sched_barrier(0);
#pragma unroll
    for (int s = 0; s < 8; ++s) {
      s0 = MFMA32(kf[2 * s], qf[s], s0);
      s1 = MFMA32(kf[2 * s + 1], qf[s], s1);
    }
    const unsigned vb0 = kb0 + 16384 + vrd;
    s16x4 vl[16], vh[16];
#pragma unroll
    for (int db = 0; db < 4; ++db) {
#pragma unroll
      for (int ks = 0; ks < 4; ++ks) {
        const unsigned a0 = (vb0 + ks * 16 * 256) ^ (db * 64);
        vl[db * 4 + ks] = trread(a0);
        vh[db * 4 + ks] = trread(a0 + 8 * 256);
      }
    }
    __builtin_amdgcn_sched_barrier(0);
    float pm = s0[0];
#pragma unroll
    for (int r = 1; r < 16; ++r) pm = fmaxf(pm, s0[r]);
#pragma unroll
    for (int r = 0; r < 16; ++r) pm = fmaxf(pm, s1[r]);
    pm = fmaxf(pm, __shfl_xor(pm, 32));
    const float mn = fmaxf(m, pm);
    const float alpha = __builtin_amdgcn_exp2f((m - mn) * C);
    m = mn;
    const float mc = -mn * C;
    float ps = 0.f;
#pragma unroll
    for (int r = 0; r < 16; ++r) { s0[r] = __builtin_amdgcn_exp2f(fmaf(s0[r], C, mc)); ps += s0[r]; }
#pragma unroll
    for (int r = 0; r < 16; ++r) { s1[r] = __builtin_amdgcn_exp2f(fmaf(s1[r], C, mc)); ps += s1[r]; }
    l = l * alpha + ps;
#pragma unroll
    for (int db = 0; db < 4; ++db)
#pragma unroll
      for (int r = 0; r < 16; ++r) o[db][r] *= alpha;
    bf16x8 pf[4];
    pf[0] = pack8(s0, 0); pf[1] = pack8(s0, 1); pf[2] = pack8(s1, 0); pf[3] = pack8(s1, 1);
#pragma unroll
    for (int db = 0; db < 4; ++db) {
#pragma unroll
      for (int ks = 0; ks < 4; ++ks) {
        bf16x8 a = __builtin_shufflevector(vl[db * 4 + ks], vh[db * 4 + ks], 0, 1, 2, 3, 4, 5, 6, 7);
        o[db] = MFMA32(a, pf[ks], o[db]);
      }
    }
  }
  l += __shfl_xor(l, 32);
  const float rl = 1.0f / l;
  short* orow = ows + ((long)(b * SQ + wid * 32 + r32)) * HD + h * DH + 4 * hh;
#pragma unroll
  for (int db = 0; db < 4; ++db) {
#pragma unroll
    for (int g = 0; g < 4; ++g) {
      u32x2 w;
      w[0] = cvtpk(o[db][4 * g + 0] * rl, o[db][4 * g + 1] * rl);
      w[1] = cvtpk(o[db][4 * g + 2] * rl, o[db][4 * g + 3] * rl);
      *(u32x2*)(orow + db * 32 + g * 8) = w;
    }
  }
}

constexpr int OP_PITCH = 208;
__global__ __launch_bounds__(256) void oproj_kernel(const short* __restrict__ ows, const short* __restrict__ wo,
                                                    float* __restrict__ part) {
  __shared__ __attribute__((aligned(16))) char smem[2][64 * OP_PITCH];
  const int tid = threadIdx.x, lane = tid & 63, wid = tid >> 6;
  const int m0 = blockIdx.x * 128 + wid * 32, n0 = blockIdx.y * 96, kbase = blockIdx.z * 1024;
  const int r32 = lane & 31, hh = lane >> 5, g1 = (lane >> 4) & 1, q4 = (lane >> 2) & 3, p4 = lane & 3;
  f32x16 acc[3] = {};
  const short* asrc = ows + (long)(m0 + r32) * HD + kbase + hh * 8;
  int srow[3], sch[3];
#pragma unroll
  for (int i = 0; i < 3; ++i) { const int idx = tid + i * 256; srow[i] = idx / 12; sch[i] = idx % 12; }
  u32x4 st[3]; bf16x8 af[4];
#pragma unroll
  for (int i = 0; i < 3; ++i) st[i] = *(const u32x4*)(wo + (long)(kbase + srow[i]) * DM + n0 + sch[i] * 8);
#pragma unroll
  for (int s2 = 0; s2 < 4; ++s2) af[s2] = *(const bf16x8*)(asrc + s2 * 16);
#pragma unroll 1
  for (int kt = 0; kt < 16; ++kt) {
    char* sb = smem[kt & 1];
#pragma unroll
    for (int i = 0; i < 3; ++i) *(u32x4*)(sb + srow[i] * OP_PITCH + sch[i] * 16) = st[i];
    bf16x8 a[4];
#pragma unroll
    for (int s2 = 0; s2 < 4; ++s2) a[s2] = af[s2];
    __syncthreads();
    if (kt + 1 < 16) {
#pragma unroll
      for (int i = 0; i < 3; ++i) st[i] = *(const u32x4*)(wo + (long)(kbase + (kt + 1) * 64 + srow[i]) * DM + n0 + sch[i] * 8);
#pragma unroll
      for (int s2 = 0; s2 < 4; ++s2) af[s2] = *(const bf16x8*)(asrc + (kt + 1) * 64 + s2 * 16);
    }
    const unsigned lb = (unsigned)(uintptr_t)sb + (8 * hh + q4) * OP_PITCH + (16 * g1 + 4 * p4) * 2;
#pragma unroll
    for (int s2 = 0; s2 < 4; ++s2) {
#pragma unroll
      for (int nb = 0; nb < 3; ++nb) {
        const unsigned ad = lb + s2 * 16 * OP_PITCH + nb * 64;
        s16x4 lo = trread(ad), hi = trread(ad + 4 * OP_PITCH);
        bf16x8 bb = __builtin_shufflevector(lo, hi, 0, 1, 2, 3, 4, 5, 6, 7);
        acc[nb] = MFMA32(a[s2], bb, acc[nb]);
      }
    }
  }
  float* pp = part + (long)blockIdx.z * (NB * SQ * DM);
#pragma unroll
  for (int nb = 0; nb < 3; ++nb)
#pragma unroll
    for (int r = 0; r < 16; ++r) {
      const int row = (r & 3) + 8 * (r >> 2) + 4 * hh;
      pp[(long)(m0 + row) * DM + n0 + nb * 32 + r32] = acc[nb][r];
    }
}
__global__ __launch_bounds__(256) void oreduce_kernel(const float* __restrict__ part, short* __restrict__ out) {
  const int i = (blockIdx.x * 256 + threadIdx.x) * 4;
  constexpr int NE = NB * SQ * DM;
  f32x4 a = *(const f32x4*)(part + i);
  a += *(const f32x4*)(part + NE + i);
  a += *(const f32x4*)(part + 2 * NE + i);
  a += *(const f32x4*)(part + 3 * NE + i);
  u32x2 w; w[0] = cvtpk(a[0], a[1]); w[1] = cvtpk(a[2], a[3]);
  *(u32x2*)(out + i) = w;
}

extern "C" void kernel_launch(void* const* d_in, const int* in_sizes, int n_in, void* d_out, int out_size, void* d_ws,
                              size_t ws_size, hipStream_t stream) {
  const short* x = (const short*)d_in[0];
  const short* kc = (const short*)d_in[1];
  const short* vc = (const short*)d_in[2];
  const short* wq = (const short*)d_in[3];
  const short* wo = (const short*)d_in[4];
  short* ows = (short*)d_ws;
  hipFuncSetAttribute((const void*)attn_kernel, hipFuncAttributeMaxDynamicSharedMemorySize, LDS_BYTES);
  attn_kernel<<<NB * NH, 512, LDS_BYTES, stream>>>(x, kc, vc, wq, ows);
  float* part = (float*)((char*)d_ws + (size_t)16 * 1024 * 1024);
  oproj_kernel<<<dim3(8, 6, 4), 256, 0, stream>>>(ows, wo, part);
  oreduce_kernel<<<NB * SQ * DM / 1024, 256, 0, stream>>>(part, (short*)d_out);
}
```

```cpp
#include <hip/hip_runtime.h>
#include <hip/hip_bf16.h>

constexpr int NB = 8, SQ = 128, DM = 576, NH = 32, SK = 1024, DH = 128;
constexpr int HD = NH * DH;

using bf16x8 = __attribute__((ext_vector_type(8))) short;
using s16x4 = __attribute__((ext_vector_type(4))) short;
using f32x16 = __attribute__((ext_vector_type(16))) float;
using f32x4 = __attribute__((ext_vector_type(4))) float;
using u32x4 = __attribute__((ext_vector_type(4))) unsigned;
using u32x2 = __attribute__((ext_vector_type(2))) unsigned;
typedef __attribute__((address_space(3))) s16x4* lds_s16x4_ptr;
typedef __attribute__((address_space(3))) void* lds_void_ptr;

#define MFMA32(a, b, c) __builtin_amdgcn_mfma_f32_32x32x16_bf16((a), (b), (c), 0, 0, 0)
#define FENCE() asm volatile("" ::: "memory")
#define BARRIER() do { asm volatile("s_waitcnt lgkmcnt(0)" ::: "memory"); __builtin_amdgcn_s_barrier(); FENCE(); } while (0)

__device__ __forceinline__ unsigned cvtpk(float lo, float hi) {
  typedef __attribute__((ext_vector_type(2))) __bf16 bf2;
  bf2 v = {(__bf16)lo, (__bf16)hi}; return __builtin_bit_cast(unsigned, v);
}
__device__ __forceinline__ bf16x8 pack8(const f32x16& x, int s) {
  u32x4 p;
  p[0] = cvtpk(x[8 * s + 0], x[8 * s + 1]); p[1] = cvtpk(x[8 * s + 2], x[8 * s + 3]);
  p[2] = cvtpk(x[8 * s + 4], x[8 * s + 5]); p[3] = cvtpk(x[8 * s + 6], x[8 * s + 7]);
  return __builtin_bit_cast(bf16x8, p);
}
__device__ __forceinline__ s16x4 trread(unsigned addr) {
  return __builtin_amdgcn_ds_read_tr16_b64_v4i16((lds_s16x4_ptr)(uintptr_t)addr);
}
__device__ __forceinline__ void glds16(const void* g, unsigned lds_addr) {
  __builtin_amdgcn_global_load_lds((const unsigned*)g, (lds_void_ptr)(uintptr_t)lds_addr, 16, 0, 0);
}

constexpr int KV_SLOT = 32768, RING = 4;
constexpr int WQ_BASE = KV_SLOT * RING;
constexpr int LDS_BYTES = WQ_BASE + 2 * 16384;
constexpr int NT = SK / 64;
constexpr int NCH = DM / 64;

__global__ __launch_bounds__(512, 1) void attn_kernel(const short* __restrict__ x, const short* __restrict__ kc,
                                                      const short* __restrict__ vc, const short* __restrict__ wq,
                                                      short* __restrict__ ows) {
  extern __shared__ __attribute__((aligned(16))) char smem[];
  const unsigned lds0 = (unsigned)(uintptr_t)smem;
  const int tid = threadIdx.x, lane = tid & 63;
  const int wid = __builtin_amdgcn_readfirstlane(tid >> 6);
  const int bid = blockIdx.x;
  const int h = (bid & 7) * 4 + ((bid >> 3) & 3), b = bid >> 5;

  if (wid >= 4) {
    const int lw = wid - 4;
    const long kvbase = ((long)(b * NH + h)) * SK * DH;
    const int rloc = lane >> 4, chp = lane & 15;
    const short* ksrc[4]; const short* vsrc[4];
#pragma unroll
    for (int i = 0; i < 4; ++i) {
      const int row = (lw * 4 + i) * 4 + rloc;
      ksrc[i] = kc + kvbase + row * DH + ((chp ^ (row & 15)) * 8);
      vsrc[i] = vc + kvbase + row * DH + ((chp ^ ((row & 3) << 2)) * 8);
    }
    const int rot = (bid * 5) & 15;
    auto issue = [&](int t) {
      const unsigned dst = lds0 + (t & 3) * KV_SLOT + lw * 4096;
#pragma unroll
      for (int i = 0; i < 4; ++i) glds16(ksrc[i] + ((t + rot) & 15) * 64 * DH, dst + i * 1024);
#pragma unroll
      for (int i = 0; i < 4; ++i) glds16(vsrc[i] + ((t + rot) & 15) * 64 * DH, dst + 16384 + i * 1024);
    };
    {
      const short* wsrc[4];
#pragma unroll
      for (int i = 0; i < 4; ++i) {
        const int row = (lw * 4 + i) * 4 + rloc;
        wsrc[i] = wq + (long)row * HD + h * DH + ((chp ^ ((row & 3) << 2)) * 8);
      }
#pragma unroll
      for (int c = 0; c < NCH; ++c) {
        const unsigned dst = lds0 + c * 16384 + lw * 4096;
#pragma unroll
        for (int i = 0; i < 4; ++i) glds16(wsrc[i] + (long)c * 64 * HD, dst + i * 1024);
      }
    }
#define LB(n) asm volatile("s_waitcnt vmcnt(" #n ")" ::: "memory"); __builtin_amdgcn_s_barrier(); FENCE();
    LB(32) LB(28) LB(24) issue(0);
    LB(28) LB(24) issue(1);
    LB(28) LB(24) issue(2);
    LB(28) LB(24) issue(3);
    asm volatile("s_waitcnt vmcnt(24)" ::: "memory"); __builtin_amdgcn_s_barrier(); FENCE();
#pragma unroll 1
    for (int t = 1; t <= 12; ++t) {
      asm volatile("s_waitcnt vmcnt(16)" ::: "memory"); __builtin_amdgcn_s_barrier(); FENCE();
      issue(t + 3);
    }
    asm volatile("s_waitcnt vmcnt(16)" ::: "memory"); __builtin_amdgcn_s_barrier(); FENCE();
    asm volatile("s_waitcnt vmcnt(8)" ::: "memory"); __builtin_amdgcn_s_barrier(); FENCE();
    asm volatile("s_waitcnt vmcnt(0)" ::: "memory"); __builtin_amdgcn_s_barrier(); FENCE();
    return;
  }

  const int r32 = lane & 31, hh = lane >> 5;
  const int g1 = (lane >> 4) & 1, q4 = (lane >> 2) & 3, p4 = lane & 3;
  bf16x8 qf[8];
  {
    const short* xsrc = x + ((long)(b * SQ + wid * 32 + r32)) * DM + hh * 8;
    bf16x8 xq[3][4];
#pragma unroll
    for (int c = 0; c < 3; ++c)
#pragma unroll
      for (int s = 0; s < 4; ++s) xq[c][s] = *(const bf16x8*)(xsrc + c * 64 + s * 16);
    f32x16 acc[4] = {};
    const int psw = ((p4 & 1) << 1) | (p4 >> 1);
    const unsigned wrd = (unsigned)((8 * hh + q4) * 256 + (((2 * g1 + (psw >> 1)) ^ (q4 << 2)) * 16) + (psw & 1) * 8);
#pragma unroll
    for (int c = 0; c < NCH; ++c) {
      BARRIER();
      bf16x8 xf[4];
#pragma unroll
      for (int s = 0; s < 4; ++s) xf[s] = xq[c % 3][s];
      if (c + 3 < NCH) {
#pragma unroll
        for (int s = 0; s < 4; ++s) xq[c % 3][s] = *(const bf16x8*)(xsrc + (c + 3) * 64 + s * 16);
      }
      const unsigned wb = lds0 + c * 16384 + wrd;
      s16x4 wl[16], wh[16];
#pragma unroll
      for (int s = 0; s < 4; ++s) {
#pragma unroll
        for (int db = 0; db < 4; ++db) {
          const unsigned a0 = wb + s * 16 * 256;
          wl[s * 4 + db] = trread((a0 ^ (db * 64)));
          wh[s * 4 + db] = trread((a0 ^ (db * 64)) + 4 * 256);
        }
      }
      __builtin_amdgcn_sched_barrier(0);
#pragma unroll
      for (int s = 0; s < 4; ++s) {
#pragma unroll
        for (int db = 0; db < 4; ++db) {
          bf16x8 a = __builtin_shufflevector(wl[s * 4 + db], wh[s * 4 + db], 0, 1, 2, 3, 4, 5, 6, 7);
          acc[db] = MFMA32(a, xf[s], acc[db]);
        }
      }
    }
#pragma unroll
    for (int db = 0; db < 4; ++db) { qf[2 * db] = pack8(acc[db], 0); qf[2 * db + 1] = pack8(acc[db], 1); }
  }

  constexpr float C = 0.088388347648318440f * 1.4426950408889634f;
  float m = -1e30f, l = 0.f;
  f32x16 o[4] = {};
  const unsigned krd = (unsigned)(r32 * 256);
  const int kx = r32 & 15;
  const unsigned vrd = (unsigned)((4 * hh + q4) * 256 + (((2 * g1 + (p4 >> 1)) ^ (q4 << 2)) * 16) + (p4 & 1) * 8);
#pragma unroll 1
  for (int t = 0; t < NT; ++t) {
    BARRIER();
    const unsigned kb0 = lds0 + (t & 3) * KV_SLOT;
    f32x16 s0 = {}, s1 = {};
    bf16x8 kf[16];
#pragma unroll
    for (int s = 0; s < 8; ++s) {
      const unsigned ca = (unsigned)(((2 * s + hh) ^ kx) * 16);
      kf[2 * s] = *(const __attribute__((address_space(3))) bf16x8*)(uintptr_t)(kb0 + krd + ca);
      kf[2 * s + 1] = *(const __attribute__((address_space(3))) bf16x8*)(uintptr_t)(kb0 + krd + 32 * 256 + ca);
    }
    __builtin_amdgcn_sched_barrier(0);
#pragma unroll
    for (int s = 0; s < 8; ++s) {
      s0 = MFMA32(kf[2 * s], qf[s], s0);
      s1 = MFMA32(kf[2 * s + 1], qf[s], s1);
    }
    const unsigned vb0 = kb0 + 16384 + vrd;
    s16x4 vl[16], vh[16];
#pragma unroll
    for (int db = 0; db < 4; ++db) {
#pragma unroll
      for (int ks = 0; ks < 4; ++ks) {
        const unsigned a0 = (vb0 + ks * 16 * 256) ^ (db * 64);
        vl[db * 4 + ks] = trread(a0);
        vh[db * 4 + ks] = trread(a0 + 8 * 256);
      }
    }
    __builtin_amdgcn_sched_barrier(0);
    float pm = s0[0];
#pragma unroll
    for (int r = 1; r < 16; ++r) pm = fmaxf(pm, s0[r]);
#pragma unroll
    for (int r = 0; r < 16; ++r) pm = fmaxf(pm, s1[r]);
    pm = fmaxf(pm, __shfl_xor(pm, 32));
    const float mn = fmaxf(m, pm);
    const float alpha = __builtin_amdgcn_exp2f((m - mn) * C);
    m = mn;
    const float mc = -mn * C;
    float ps = 0.f;
#pragma unroll
    for (int r = 0; r < 16; ++r) { s0[r] = __builtin_amdgcn_exp2f(fmaf(s0[r], C, mc)); ps += s0[r]; }
#pragma unroll
    for (int r = 0; r < 16; ++r) { s1[r] = __builtin_amdgcn_exp2f(fmaf(s1[r], C, mc)); ps += s1[r]; }
    l = l * alpha + ps;
#pragma unroll
    for (int db = 0; db < 4; ++db)
#pragma unroll
      for (int r = 0; r < 16; ++r) o[db][r] *= alpha;
    bf16x8 pf[4];
    pf[0] = pack8(s0, 0); pf[1] = pack8(s0, 1); pf[2] = pack8(s1, 0); pf[3] = pack8(s1, 1);
#pragma unroll
    for (int db = 0; db < 4; ++db) {
#pragma unroll
      for (int ks = 0; ks < 4; ++ks) {
        bf16x8 a = __builtin_shufflevector(vl[db * 4 + ks], vh[db * 4 + ks], 0, 1, 2, 3, 4, 5, 6, 7);
        o[db] = MFMA32(a, pf[ks], o[db]);
      }
    }
  }
  l += __shfl_xor(l, 32);
  const float rl = 1.0f / l;
  short* orow = ows + ((long)(b * SQ + wid * 32 + r32)) * HD + h * DH + 4 * hh;
#pragma unroll
  for (int db = 0; db < 4; ++db) {
#pragma unroll
    for (int g = 0; g < 4; ++g) {
      u32x2 w;
      w[0] = cvtpk(o[db][4 * g + 0] * rl, o[db][4 * g + 1] * rl);
      w[1] = cvtpk(o[db][4 * g + 2] * rl, o[db][4 * g + 3] * rl);
      *(u32x2*)(orow + db * 32 + g * 8) = w;
    }
  }
}

constexpr int OP_PITCH = 208;
__global__ __launch_bounds__(256) void oproj_kernel(const short* __restrict__ ows, const short* __restrict__ wo,
                                                    float* __restrict__ part) {
  __shared__ __attribute__((aligned(16))) char smem[2][64 * OP_PITCH];
  const int tid = threadIdx.x, lane = tid & 63, wid = tid >> 6;
  const int m0 = blockIdx.x * 128 + wid * 32, n0 = blockIdx.y * 96, kbase = blockIdx.z * 512;
  const int r32 = lane & 31, hh = lane >> 5, g1 = (lane >> 4) & 1, q4 = (lane >> 2) & 3, p4 = lane & 3;
  f32x16 acc[3] = {};
  const short* asrc = ows + (long)(m0 + r32) * HD + kbase + hh * 8;
  int srow[3], sch[3];
#pragma unroll
  for (int i = 0; i < 3; ++i) { const int idx = tid + i * 256; srow[i] = idx / 12; sch[i] = idx % 12; }
  u32x4 st[3]; bf16x8 af[4];
#pragma unroll
  for (int i = 0; i < 3; ++i) st[i] = *(const u32x4*)(wo + (long)(kbase + srow[i]) * DM + n0 + sch[i] * 8);
#pragma unroll
  for (int s2 = 0; s2 < 4; ++s2) af[s2] = *(const bf16x8*)(asrc + s2 * 16);
#pragma unroll 1
  for (int kt = 0; kt < 8; ++kt) {
    char* sb = smem[kt & 1];
#pragma unroll
    for (int i = 0; i < 3; ++i) *(u32x4*)(sb + srow[i] * OP_PITCH + sch[i] * 16) = st[i];
    bf16x8 a[4];
#pragma unroll
    for (int s2 = 0; s2 < 4; ++s2) a[s2] = af[s2];
    __syncthreads();
    if (kt + 1 < 8) {
#pragma unroll
      for (int i = 0; i < 3; ++i) st[i] = *(const u32x4*)(wo + (long)(kbase + (kt + 1) * 64 + srow[i]) * DM + n0 + sch[i] * 8);
#pragma unroll
      for (int s2 = 0; s2 < 4; ++s2) af[s2] = *(const bf16x8*)(asrc + (kt + 1) * 64 + s2 * 16);
    }
    const unsigned lb = (unsigned)(uintptr_t)sb + (8 * hh + q4) * OP_PITCH + (16 * g1 + 4 * p4) * 2;
#pragma unroll
    for (int s2 = 0; s2 < 4; ++s2) {
#pragma unroll
      for (int nb = 0; nb < 3; ++nb) {
        const unsigned ad = lb + s2 * 16 * OP_PITCH + nb * 64;
        s16x4 lo = trread(ad), hi = trread(ad + 4 * OP_PITCH);
        bf16x8 bb = __builtin_shufflevector(lo, hi, 0, 1, 2, 3, 4, 5, 6, 7);
        acc[nb] = MFMA32(a[s2], bb, acc[nb]);
      }
    }
  }
  float* pp = part + (long)blockIdx.z * (NB * SQ * DM);
#pragma unroll
  for (int nb = 0; nb < 3; ++nb)
#pragma unroll
    for (int r = 0; r < 16; ++r) {
      const int row = (r & 3) + 8 * (r >> 2) + 4 * hh;
      pp[(long)(m0 + row) * DM + n0 + nb * 32 + r32] = acc[nb][r];
    }
}
__global__ __launch_bounds__(256) void oreduce_kernel(const float* __restrict__ part, short* __restrict__ out) {
  const int i = (blockIdx.x * 256 + threadIdx.x) * 4;
  constexpr int NE = NB * SQ * DM;
  f32x4 a = *(const f32x4*)(part + i);
  a += *(const f32x4*)(part + NE + i);
  a += *(const f32x4*)(part + 2 * NE + i);
  a += *(const f32x4*)(part + 3 * NE + i);
  a += *(const f32x4*)(part + 4 * NE + i);
  a += *(const f32x4*)(part + 5 * NE + i);
  a += *(const f32x4*)(part + 6 * NE + i);
  a += *(const f32x4*)(part + 7 * NE + i);
  u32x2 w; w[0] = cvtpk(a[0], a[1]); w[1] = cvtpk(a[2], a[3]);
  *(u32x2*)(out + i) = w;
}

extern "C" void kernel_launch(void* const* d_in, const int* in_sizes, int n_in, void* d_out, int out_size, void* d_ws,
                              size_t ws_size, hipStream_t stream) {
  const short* x = (const short*)d_in[0];
  const short* kc = (const short*)d_in[1];
  const short* vc = (const short*)d_in[2];
  const short* wq = (const short*)d_in[3];
  const short* wo = (const short*)d_in[4];
  short* ows = (short*)d_ws;
  hipFuncSetAttribute((const void*)attn_kernel, hipFuncAttributeMaxDynamicSharedMemorySize, LDS_BYTES);
  attn_kernel<<<NB * NH, 512, LDS_BYTES, stream>>>(x, kc, vc, wq, ows);
  float* part = (float*)((char*)d_ws + (size_t)16 * 1024 * 1024);
  oproj_kernel<<<dim3(8, 6, 8), 256, 0, stream>>>(ows, wo, part);
  oreduce_kernel<<<NB * SQ * DM / 1024, 256, 0, stream>>>(part, (short*)d_out);
}
```
